```python
import torch

def _softmax(x, axis=-1):
    return torch.softmax(x, dim=axis)
def _gelu(x):
    return torch.nn.functional.gelu(x, approximate="tanh")
def _silu(x):
    return torch.nn.functional.silu(x)
def _sigmoid(x):
    return torch.sigmoid(x)
def _layernorm(x, axis=-1, eps=1e-5):
    m = x.mean(dim=axis, keepdim=True)
    v = x.var(dim=axis, keepdim=True, unbiased=False)
    return (x - m) / torch.sqrt(v + eps)
def _scatter_add(tgt, idx, src, axis=0):
    return tgt.clone().index_add_(axis, idx, src)
def _segment_sum(src, seg, n):
    out = torch.zeros((n,) + src.shape[1:], dtype=src.dtype)
    return out.index_add_(0, seg, src)
def _pad(x, pw):
    flat = tuple(v for pair in reversed(pw) for v in pair)
    return torch.nn.functional.pad(x, flat)

kernel_name = 'standalone__fm_second_order__recsys'

def setup_inputs():

    _w = (lambda a: (lambda t: t.to(torch.bfloat16) if t.is_floating_point() else t)(torch.as_tensor(a)))
    B, F, D, V = 4096, 26, 128, min(1000000, 100000)
    return {
        "idx": _w((lambda lo, hi, *s: torch.randint(lo, hi, s))(0, V, B, F).to(torch.int64)),
        "table": _w((lambda *s: torch.randn(*s, dtype=torch.bfloat16) * 0.05)(V, D)),
    }

def reference(idx, table):
    emb = table[idx]
    sum_sq = emb.sum(axis=1) ** 2
    sq_sum = (emb ** 2).sum(axis=1)
    return 0.5 * (sum_sq - sq_sum).sum(axis=-1)
```

```cpp
#include <hip/hip_runtime.h>
#include <hip/hip_bf16.h>
#include <stdint.h>

typedef __attribute__((ext_vector_type(4))) unsigned int u32x4;
typedef __attribute__((ext_vector_type(2))) unsigned int u32x2;
typedef __attribute__((ext_vector_type(2))) float f32x2;
typedef __attribute__((ext_vector_type(2))) __bf16 bf16x2_t;

#define F_ 26
#define D_ 128
#define MODE 1
#define BLOCK 512

__device__ __forceinline__ float bf_lo(unsigned u) { return __uint_as_float(u << 16); }
__device__ __forceinline__ float bf_hi(unsigned u) { return __uint_as_float(u & 0xffff0000u); }

#define DPP_ADD(v, ctrl) v += __int_as_float(__builtin_amdgcn_update_dpp(0, __float_as_int(v), ctrl, 0xf, 0xf, true))

__device__ __forceinline__ float row16_sum(float v) {
  DPP_ADD(v, 0x128);
  DPP_ADD(v, 0x124);
  DPP_ADD(v, 0x122);
  DPP_ADD(v, 0x121);
  return v;
}
__device__ __forceinline__ float dot2(unsigned u, float acc) {
  bf16x2_t a = __builtin_bit_cast(bf16x2_t, u);
  return __builtin_amdgcn_fdot2_f32_bf16(a, a, acc, false);
}

constexpr int SPB = BLOCK / 32;
__global__ __launch_bounds__(BLOCK) void fm_kernel(const int* __restrict__ idx, const unsigned char* __restrict__ table,
                                                 __hip_bfloat16* __restrict__ out) {
  const int tid = threadIdx.x;
  const unsigned g = blockIdx.x * (BLOCK / 16) + (tid >> 4);
  const unsigned c = tid & 15;
  const int* ip = idx + g * 13;
  unsigned ix[13];
#pragma unroll
  for (int f = 0; f < 13; ++f) ix[f] = (unsigned)ip[f];
  u32x4 r[13];
#pragma unroll
  for (int f = 0; f < 13; ++f) {
    unsigned off = ix[f] * 256u + c * 16u;
    r[f] = *(const u32x4*)(table + off);
  }
  __builtin_amdgcn_sched_barrier(0);
  f32x2 s[4];
#pragma unroll
  for (int j = 0; j < 4; ++j) s[j] = f32x2{0.f, 0.f};
  float q0 = 0.f, q1 = 0.f;
#pragma unroll
  for (int f = 0; f < 13; ++f) {
#pragma unroll
    for (int j = 0; j < 4; ++j) {
      unsigned u = r[f][j];
      s[j] += f32x2{bf_lo(u), bf_hi(u)};
      if (j & 1) q1 = dot2(u, q1); else q0 = dot2(u, q0);
    }
  }
  float t = -(q0 + q1);
#pragma unroll
  for (int j = 0; j < 4; ++j) {
#pragma unroll
    for (int k = 0; k < 2; ++k) {
      float v = s[j][k];
      auto sw = __builtin_amdgcn_permlane16_swap(__float_as_uint(v), __float_as_uint(v), false, false);
      float tot = __uint_as_float(sw[0]) + __uint_as_float(sw[1]);
      t = fmaf(0.5f * tot, tot, t);
    }
  }
  t = row16_sum(t);
  auto sw = __builtin_amdgcn_permlane16_swap(__float_as_uint(t), __float_as_uint(t), false, false);
  t = __uint_as_float(sw[0]) + __uint_as_float(sw[1]);
  if ((tid & 31) == 0) out[g >> 1] = __float2bfloat16(0.5f * t);
}

extern "C" void kernel_launch(void* const* d_in, const int* in_sizes, int n_in, void* d_out, int out_size, void* d_ws,
                              size_t ws_size, hipStream_t stream) {
  const int* idx = (const int*)d_in[0];
  const unsigned char* table = (const unsigned char*)d_in[1];
  int B = out_size;
  fm_kernel<<<B / SPB, BLOCK, 0, stream>>>(idx, table, (__hip_bfloat16*)d_out);
}
```

```cpp
#include <hip/hip_runtime.h>
#include <hip/hip_bf16.h>
#include <stdint.h>
#include <stdio.h>

typedef __attribute__((ext_vector_type(4))) unsigned int u32x4;
typedef __attribute__((ext_vector_type(2))) unsigned int u32x2;
typedef __attribute__((ext_vector_type(2))) float f32x2;
typedef __attribute__((ext_vector_type(2))) __bf16 bf16x2_t;

#define F_ 26
#define D_ 128
#define MODE 1
#define BLOCK 512

__device__ __forceinline__ float bf_lo(unsigned u) { return __uint_as_float(u << 16); }
__device__ __forceinline__ float bf_hi(unsigned u) { return __uint_as_float(u & 0xffff0000u); }

#define DPP_ADD(v, ctrl) v += __int_as_float(__builtin_amdgcn_update_dpp(0, __float_as_int(v), ctrl, 0xf, 0xf, true))

__device__ __forceinline__ float row16_sum(float v) {
  DPP_ADD(v, 0x128);
  DPP_ADD(v, 0x124);
  DPP_ADD(v, 0x122);
  DPP_ADD(v, 0x121);
  return v;
}
__device__ __forceinline__ float dot2(unsigned u, float acc) {
  bf16x2_t a = __builtin_bit_cast(bf16x2_t, u);
  return __builtin_amdgcn_fdot2_f32_bf16(a, a, acc, false);
}

constexpr int SPB = BLOCK / 32;
__global__ __launch_bounds__(BLOCK) void fm_kernel(const int* __restrict__ idx, const unsigned char* __restrict__ table,
                                                 __hip_bfloat16* __restrict__ out) {
  const int tid = threadIdx.x;
  const unsigned g = blockIdx.x * (BLOCK / 16) + (tid >> 4);
  const unsigned c = tid & 15;
  const int* ip = idx + g * 13;
  unsigned ix[13];
#pragma unroll
  for (int f = 0; f < 13; ++f) ix[f] = (unsigned)ip[f];
  u32x4 r[13];
#pragma unroll
  for (int f = 0; f < 13; ++f) {
    unsigned off = ix[f] * 256u + c * 16u;
    r[f] = *(const u32x4*)(table + off);
  }
  __builtin_amdgcn_sched_barrier(0);
  f32x2 s[4];
#pragma unroll
  for (int j = 0; j < 4; ++j) s[j] = f32x2{0.f, 0.f};
  float q0 = 0.f, q1 = 0.f;
#pragma unroll
  for (int f = 0; f < 13; ++f) {
#pragma unroll
    for (int j = 0; j < 4; ++j) {
      unsigned u = r[f][j];
      s[j] += f32x2{bf_lo(u), bf_hi(u)};
      if (j & 1) q1 = dot2(u, q1); else q0 = dot2(u, q0);
    }
  }
  float t = -(q0 + q1);
#pragma unroll
  for (int j = 0; j < 4; ++j) {
#pragma unroll
    for (int k = 0; k < 2; ++k) {
      float v = s[j][k];
      auto sw = __builtin_amdgcn_permlane16_swap(__float_as_uint(v), __float_as_uint(v), false, false);
      float tot = __uint_as_float(sw[0]) + __uint_as_float(sw[1]);
      t = fmaf(0.5f * tot, tot, t);
    }
  }
  t = row16_sum(t);
  auto sw = __builtin_amdgcn_permlane16_swap(__float_as_uint(t), __float_as_uint(t), false, false);
  t = __uint_as_float(sw[0]) + __uint_as_float(sw[1]);
  if ((tid & 31) == 0) out[g >> 1] = __float2bfloat16(0.5f * t);
}

extern "C" void kernel_launch(void* const* d_in, const int* in_sizes, int n_in, void* d_out, int out_size, void* d_ws,
                              size_t ws_size, hipStream_t stream) {
  const int* idx = (const int*)d_in[0];
  const unsigned char* table = (const unsigned char*)d_in[1];
  int B = out_size;
  for (int i = 0; i < 4; ++i) {
    void* p = i < 2 ? d_in[i] : (i == 2 ? d_out : d_ws);
    hipPointerAttribute_t at; hipError_t e = hipPointerGetAttributes(&at, p);
    hipDeviceptr_t base = 0; size_t sz = 0; hipError_t e2 = hipMemGetAddressRange(&base, &sz, p);
    fprintf(stderr, "ptr %d %p err %d type %d dev %d managed %d flags %u | base %p size %zu err %d\n", i, p, (int)e, (int)at.type, at.device, at.isManaged, at.allocationFlags, base, sz, (int)e2);
  }
  fm_kernel<<<B / SPB, BLOCK, 0, stream>>>(idx, table, (__hip_bfloat16*)d_out);
}
```

```cpp
#include <hip/hip_runtime.h>
#include <hip/hip_bf16.h>
#include <stdint.h>
#include <stdio.h>
#include <vector>
#include <algorithm>

typedef __attribute__((ext_vector_type(4))) unsigned int u32x4;
typedef __attribute__((ext_vector_type(2))) unsigned int u32x2;
typedef __attribute__((ext_vector_type(2))) float f32x2;
typedef __attribute__((ext_vector_type(2))) __bf16 bf16x2_t;

#define F_ 26
#define D_ 128
#define BLOCK 512
#define TOUCH 0

__device__ __forceinline__ float bf_lo(unsigned u) { return __uint_as_float(u << 16); }
__device__ __forceinline__ float bf_hi(unsigned u) { return __uint_as_float(u & 0xffff0000u); }
#define DPP_ADD(v, ctrl) v += __int_as_float(__builtin_amdgcn_update_dpp(0, __float_as_int(v), ctrl, 0xf, 0xf, true))
__device__ __forceinline__ float row16_sum(float v) {
  DPP_ADD(v, 0x128); DPP_ADD(v, 0x124); DPP_ADD(v, 0x122); DPP_ADD(v, 0x121);
  return v;
}
__device__ __forceinline__ float dot2(unsigned u, float acc) {
  bf16x2_t a = __builtin_bit_cast(bf16x2_t, u);
  return __builtin_amdgcn_fdot2_f32_bf16(a, a, acc, false);
}
#define STAMP(t) do { __builtin_amdgcn_sched_barrier(0); asm volatile("s_memrealtime %0\n\ts_waitcnt lgkmcnt(0)" : "=s"(t) :: "memory"); __builtin_amdgcn_sched_barrier(0); } while (0)

constexpr int SPB = BLOCK / 32;
__global__ __launch_bounds__(BLOCK) void fm_kernel(const int* __restrict__ idx, const unsigned char* __restrict__ table,
                                                 __hip_bfloat16* __restrict__ out, unsigned long long* __restrict__ st) {
  unsigned long long t0, t1, t2, t3, t4, t5;
  STAMP(t0);
  const int tid = threadIdx.x;
  const unsigned g = blockIdx.x * (BLOCK / 16) + (tid >> 4);
  const unsigned c = tid & 15;
  const int* ip = idx + g * 13;
  asm volatile("" :: "s"(idx), "s"(table), "s"(out));
  STAMP(t1);
  unsigned ix[13];
#pragma unroll
  for (int f = 0; f < 13; ++f) ix[f] = (unsigned)ip[f];
  __builtin_amdgcn_sched_barrier(0);
  asm volatile("" :: "v"(ix[0]), "v"(ix[12]));
  STAMP(t2);
  u32x4 r[13];
#pragma unroll
  for (int f = 0; f < 13; ++f) {
    unsigned off = ix[f] * 256u + c * 16u;
    r[f] = *(const u32x4*)(table + off);
  }
  __builtin_amdgcn_sched_barrier(0);
  asm volatile("" :: "v"(r[0]));
  STAMP(t3);
  f32x2 s[4];
#pragma unroll
  for (int j = 0; j < 4; ++j) s[j] = f32x2{0.f, 0.f};
  float q0 = 0.f, q1 = 0.f;
#pragma unroll
  for (int f = 0; f < 13; ++f) {
#pragma unroll
    for (int j = 0; j < 4; ++j) {
      unsigned u = r[f][j];
      s[j] += f32x2{bf_lo(u), bf_hi(u)};
      if (j & 1) q1 = dot2(u, q1); else q0 = dot2(u, q0);
    }
  }
  float t = -(q0 + q1);
#pragma unroll
  for (int j = 0; j < 4; ++j) {
#pragma unroll
    for (int k = 0; k < 2; ++k) {
      float v = s[j][k];
      auto sw = __builtin_amdgcn_permlane16_swap(__float_as_uint(v), __float_as_uint(v), false, false);
      float tot = __uint_as_float(sw[0]) + __uint_as_float(sw[1]);
      t = fmaf(0.5f * tot, tot, t);
    }
  }
  t = row16_sum(t);
  auto sw = __builtin_amdgcn_permlane16_swap(__float_as_uint(t), __float_as_uint(t), false, false);
  t = __uint_as_float(sw[0]) + __uint_as_float(sw[1]);
  asm volatile("" :: "v"(t));
  STAMP(t4);
  if ((tid & 31) == 0) out[g >> 1] = __float2bfloat16(0.5f * t);
  asm volatile("s_waitcnt vmcnt(0)" ::: "memory");
  STAMP(t5);
  if (blockIdx.x == 0 && __builtin_amdgcn_readfirstlane(tid >> 6) == 0) {
   { unsigned long long tend = t0 + 2000 + 5 * 0; while (__builtin_amdgcn_s_memrealtime() < tend) __builtin_amdgcn_s_sleep(1); }
 }
}

extern "C" void kernel_launch(void* const* d_in, const int* in_sizes, int n_in, void* d_out, int out_size, void* d_ws,
                              size_t ws_size, hipStream_t stream) {
  const int* idx = (const int*)d_in[0];
  const unsigned char* table = (const unsigned char*)d_in[1];
  int B = out_size;
  fm_kernel<<<B / SPB, BLOCK, 0, stream>>>(idx, table, (__hip_bfloat16*)d_out, (unsigned long long*)d_ws);
}
```

```cpp
#include <hip/hip_runtime.h>
#include <hip/hip_bf16.h>
#include <stdint.h>

typedef __attribute__((ext_vector_type(4))) unsigned int u32x4;
typedef __attribute__((ext_vector_type(2))) float f32x2;
typedef __attribute__((ext_vector_type(2))) __bf16 bf16x2_t;

#define BLOCK 512
#define AUX 17
#define RPL 13

__device__ __forceinline__ float bf_lo(unsigned u) { return __uint_as_float(u << 16); }
__device__ __forceinline__ float bf_hi(unsigned u) { return __uint_as_float(u & 0xffff0000u); }
#define DPP_ADD(v, ctrl) v += __int_as_float(__builtin_amdgcn_update_dpp(0, __float_as_int(v), ctrl, 0xf, 0xf, true))
__device__ __forceinline__ float row16_sum(float v) {
  DPP_ADD(v, 0x128); DPP_ADD(v, 0x124); DPP_ADD(v, 0x122); DPP_ADD(v, 0x121);
  return v;
}
__device__ __forceinline__ float dot2(unsigned u, float acc) {
  bf16x2_t a = __builtin_bit_cast(bf16x2_t, u);
  return __builtin_amdgcn_fdot2_f32_bf16(a, a, acc, false);
}

__global__ __launch_bounds__(BLOCK) void fm_kernel(const int* __restrict__ idx, const unsigned char* __restrict__ table,
                                                 __hip_bfloat16* __restrict__ out) {
  const int tid = threadIdx.x;
  const unsigned g = blockIdx.x * (BLOCK / 16) + (tid >> 4);
  const unsigned c = tid & 15;
  const int* ip = idx + g * RPL;
  unsigned ix[RPL];
#pragma unroll
  for (int f = 0; f < RPL; ++f) ix[f] = (unsigned)ip[f];
  auto rsrc = __builtin_amdgcn_make_buffer_rsrc((void*)table, 0, 25600000, 0x00020000);
  u32x4 r[RPL];
#pragma unroll
  for (int f = 0; f < RPL; ++f) {
    unsigned off = ix[f] * 256u + c * 16u;
    r[f] = __builtin_bit_cast(u32x4, __builtin_amdgcn_raw_buffer_load_b128(rsrc, off, 0, AUX));
  }
  __builtin_amdgcn_sched_barrier(0);
  f32x2 s[4];
#pragma unroll
  for (int j = 0; j < 4; ++j) s[j] = f32x2{0.f, 0.f};
  float q0 = 0.f, q1 = 0.f;
#pragma unroll
  for (int f = 0; f < RPL; ++f) {
#pragma unroll
    for (int j = 0; j < 4; ++j) {
      unsigned u = r[f][j];
      s[j] += f32x2{bf_lo(u), bf_hi(u)};
      if (j & 1) q1 = dot2(u, q1); else q0 = dot2(u, q0);
    }
  }
  float t = -(q0 + q1);
#pragma unroll
  for (int j = 0; j < 4; ++j) {
#pragma unroll
    for (int k = 0; k < 2; ++k) {
      float v = s[j][k];
      auto sw = __builtin_amdgcn_permlane16_swap(__float_as_uint(v), __float_as_uint(v), false, false);
      float tot = __uint_as_float(sw[0]) + __uint_as_float(sw[1]);
      t = fmaf(0.5f * tot, tot, t);
    }
  }
  t = row16_sum(t);
  auto sw = __builtin_amdgcn_permlane16_swap(__float_as_uint(t), __float_as_uint(t), false, false);
  t = __uint_as_float(sw[0]) + __uint_as_float(sw[1]);
  if ((tid & 31) == 0) out[g >> 1] = __float2bfloat16(0.5f * t);
}

extern "C" void kernel_launch(void* const* d_in, const int* in_sizes, int n_in, void* d_out, int out_size, void* d_ws,
                              size_t ws_size, hipStream_t stream) {
  const int* idx = (const int*)d_in[0];
  const unsigned char* table = (const unsigned char*)d_in[1];
  int B = out_size;
  fm_kernel<<<B * 32 / BLOCK, BLOCK, 0, stream>>>(idx, table, (__hip_bfloat16*)d_out);
}
```

```cpp
#include <hip/hip_runtime.h>
#include <hip/hip_bf16.h>
#include <stdint.h>

typedef __attribute__((ext_vector_type(4))) unsigned int u32x4;
typedef __attribute__((ext_vector_type(4))) float f32x4;
typedef __attribute__((ext_vector_type(8))) __bf16 bf16x8_t;

#define BLOCK 512
#define AUX 0
#define SAUX 0
#define SWZ 0
#define NF 26

#define DPP_ADD(v, ctrl) v += __int_as_float(__builtin_amdgcn_update_dpp(0, __float_as_int(v), ctrl, 0xf, 0xf, true))

template <int K> __device__ __forceinline__ unsigned bcast_or(unsigned sh, unsigned c16) {
  return (unsigned)__builtin_amdgcn_update_dpp(0, (int)sh, 0x150 + K, 0xf, 0xf, true) | c16;
}

__global__ __launch_bounds__(BLOCK) void fm_kernel(const int* __restrict__ idx, const unsigned char* __restrict__ table,
                                                 __hip_bfloat16* __restrict__ out, unsigned table_bytes, unsigned nb) {
  const unsigned tid = threadIdx.x;
  unsigned blk = blockIdx.x;
  const unsigned b = blk * (BLOCK / 64) + (tid >> 6);
  const unsigned lane = tid & 63, c = tid & 15, g = lane >> 4;
  const auto trs = __builtin_amdgcn_make_buffer_rsrc((void*)table, 0, table_bytes, 0x00020000);
  const auto irs = __builtin_amdgcn_make_buffer_rsrc((void*)idx, 0, nb * (NF * 4), 0x00020000);
  const auto ors = __builtin_amdgcn_make_buffer_rsrc((void*)out, 0, nb * 2, 0x00020000);
  const unsigned f = 4 * c + g;
  const unsigned oob = f < NF ? 0u : 0x80000000u;
  const unsigned ix = __builtin_amdgcn_raw_buffer_load_b32(irs, ((b * NF + f) * 4) | oob, 0, 0);
  const unsigned sh = (ix << 8) | oob, c16 = c * 16;
  u32x4 r[7];
#define LD(k) r[k] = __builtin_bit_cast(u32x4, __builtin_amdgcn_raw_buffer_load_b128(trs, bcast_or<k>(sh, c16), 0, AUX));
  LD(0) LD(1) LD(2) LD(3) LD(4) LD(5) LD(6)
#undef LD
  __builtin_amdgcn_sched_barrier(0);
  const unsigned half = (c & 1) ? 0x3f000000u : 0x00003f00u;
  const unsigned jj = (c & 7) >> 1;
  const u32x4 a = {jj == 0 ? half : 0u, jj == 1 ? half : 0u, jj == 2 ? half : 0u, jj == 3 ? half : 0u};
  const bf16x8_t sel = __builtin_bit_cast(bf16x8_t, a);
  const unsigned di = c - 4 * g;
  const float w0 = di == 0 ? -0.5f : 0.f, w1 = di == 1 ? -0.5f : 0.f, w2 = di == 2 ? -0.5f : 0.f, w3 = di == 3 ? -0.5f : 0.f;
  const unsigned ooff = lane == 0 ? b * 2 : 0x80000000u;
  f32x4 acc = {0.f, 0.f, 0.f, 0.f}, dq = {0.f, 0.f, 0.f, 0.f};
  __builtin_amdgcn_sched_barrier(0);
#pragma unroll
  for (int k = 0; k < 7; ++k) {
    const bf16x8_t x = __builtin_bit_cast(bf16x8_t, r[k]);
    acc = __builtin_amdgcn_mfma_f32_16x16x32_bf16(sel, x, acc, 0, 0, 0);
    dq = __builtin_amdgcn_mfma_f32_16x16x32_bf16(x, x, dq, 0, 0, 0);
  }
  float t0 = acc[0] * acc[0], t1 = acc[1] * acc[1];
  t0 = fmaf(acc[2], acc[2], t0); t1 = fmaf(acc[3], acc[3], t1);
  t0 = fmaf(w0, dq[0], t0); t1 = fmaf(w1, dq[1], t1);
  t0 = fmaf(w2, dq[2], t0); t1 = fmaf(w3, dq[3], t1);
  float t = t0 + t1;
  DPP_ADD(t, 0x128); DPP_ADD(t, 0x124); DPP_ADD(t, 0x122); DPP_ADD(t, 0x121);
  const auto s16 = __builtin_amdgcn_permlane16_swap(__float_as_uint(t), __float_as_uint(t), false, false);
  t = __uint_as_float(s16[0]) + __uint_as_float(s16[1]);
  const auto s32 = __builtin_amdgcn_permlane32_swap(__float_as_uint(t), __float_as_uint(t), false, false);
  t = __uint_as_float(s32[0]) + __uint_as_float(s32[1]);
  const __hip_bfloat16 o = __float2bfloat16(t);
  __builtin_amdgcn_raw_buffer_store_b16(__builtin_bit_cast(unsigned short, o), ors, ooff, 0, SAUX);
}

extern "C" void kernel_launch(void* const* d_in, const int* in_sizes, int n_in, void* d_out, int out_size, void* d_ws,
                              size_t ws_size, hipStream_t stream) {
  const int* idx = (const int*)d_in[0];
  const unsigned char* table = (const unsigned char*)d_in[1];
  const unsigned nb = (unsigned)out_size;
  const unsigned spb = BLOCK / 64;
  unsigned grid = (nb + spb - 1) / spb;
  fm_kernel<<<grid, BLOCK, 0, stream>>>(idx, table, (__hip_bfloat16*)d_out, (unsigned)in_sizes[1] * 2u, nb);
}
```

```cpp
#include <hip/hip_runtime.h>
#include <hip/hip_bf16.h>
#include <stdint.h>

typedef __attribute__((ext_vector_type(4))) unsigned int u32x4;
typedef __attribute__((ext_vector_type(4))) float f32x4;
typedef __attribute__((ext_vector_type(8))) __bf16 bf16x8_t;

#define BLOCK 1024
#define AUX 0
#define SAUX 0
#define SWZ 0
#define NF 26

#define DPP_ADD(v, ctrl) v += __int_as_float(__builtin_amdgcn_update_dpp(0, __float_as_int(v), ctrl, 0xf, 0xf, true))

template <int K> __device__ __forceinline__ unsigned bcast_or(unsigned sh, unsigned c16) {
  return (unsigned)__builtin_amdgcn_update_dpp(0, (int)sh, 0x150 + K, 0xf, 0xf, true) | c16;
}

__global__ __launch_bounds__(BLOCK) void fm_kernel(const int* __restrict__ idx, const unsigned char* __restrict__ table,
                                                 __hip_bfloat16* __restrict__ out, unsigned table_bytes, unsigned nb, unsigned long long* __restrict__ st) {
  unsigned long long ts0; asm volatile("s_memrealtime %0" : "=s"(ts0) :: "memory");
  const unsigned tid = threadIdx.x;
  unsigned blk = blockIdx.x;
  const unsigned b = blk * (BLOCK / 64) + (tid >> 6);
  const unsigned lane = tid & 63, c = tid & 15, g = lane >> 4;
  const auto trs = __builtin_amdgcn_make_buffer_rsrc((void*)table, 0, table_bytes, 0x00020000);
  const auto irs = __builtin_amdgcn_make_buffer_rsrc((void*)idx, 0, nb * (NF * 4), 0x00020000);
  const auto ors = __builtin_amdgcn_make_buffer_rsrc((void*)out, 0, nb * 2, 0x00020000);
  const unsigned f = 4 * c + g;
  const unsigned oob = f < NF ? 0u : 0x80000000u;
  const unsigned ix = __builtin_amdgcn_raw_buffer_load_b32(irs, ((b * NF + f) * 4) | oob, 0, 0);
  const unsigned sh = (ix << 8) | oob, c16 = c * 16;
  u32x4 r[7];
#define LD(k) r[k] = __builtin_bit_cast(u32x4, __builtin_amdgcn_raw_buffer_load_b128(trs, bcast_or<k>(sh, c16), 0, AUX));
  LD(0) LD(1) LD(2) LD(3) LD(4) LD(5) LD(6)
#undef LD
  __builtin_amdgcn_sched_barrier(0);
  const unsigned half = (c & 1) ? 0x3f000000u : 0x00003f00u;
  const unsigned jj = (c & 7) >> 1;
  const u32x4 a = {jj == 0 ? half : 0u, jj == 1 ? half : 0u, jj == 2 ? half : 0u, jj == 3 ? half : 0u};
  const bf16x8_t sel = __builtin_bit_cast(bf16x8_t, a);
  const unsigned di = c - 4 * g;
  const float w0 = di == 0 ? -0.5f : 0.f, w1 = di == 1 ? -0.5f : 0.f, w2 = di == 2 ? -0.5f : 0.f, w3 = di == 3 ? -0.5f : 0.f;
  const unsigned ooff = lane == 0 ? b * 2 : 0x80000000u;
  f32x4 acc = {0.f, 0.f, 0.f, 0.f}, dq = {0.f, 0.f, 0.f, 0.f};
  __builtin_amdgcn_sched_barrier(0);
#pragma unroll
  for (int k = 0; k < 7; ++k) {
    const bf16x8_t x = __builtin_bit_cast(bf16x8_t, r[k]);
    acc = __builtin_amdgcn_mfma_f32_16x16x32_bf16(sel, x, acc, 0, 0, 0);
    dq = __builtin_amdgcn_mfma_f32_16x16x32_bf16(x, x, dq, 0, 0, 0);
  }
  float t0 = acc[0] * acc[0], t1 = acc[1] * acc[1];
  t0 = fmaf(acc[2], acc[2], t0); t1 = fmaf(acc[3], acc[3], t1);
  t0 = fmaf(w0, dq[0], t0); t1 = fmaf(w1, dq[1], t1);
  t0 = fmaf(w2, dq[2], t0); t1 = fmaf(w3, dq[3], t1);
  float t = t0 + t1;
  DPP_ADD(t, 0x128); DPP_ADD(t, 0x124); DPP_ADD(t, 0x122); DPP_ADD(t, 0x121);
  const auto s16 = __builtin_amdgcn_permlane16_swap(__float_as_uint(t), __float_as_uint(t), false, false);
  t = __uint_as_float(s16[0]) + __uint_as_float(s16[1]);
  const auto s32 = __builtin_amdgcn_permlane32_swap(__float_as_uint(t), __float_as_uint(t), false, false);
  t = __uint_as_float(s32[0]) + __uint_as_float(s32[1]);
  const __hip_bfloat16 o = __float2bfloat16(t);
  __builtin_amdgcn_raw_buffer_store_b16(__builtin_bit_cast(unsigned short, o), ors, ooff, 0, SAUX);
  unsigned long long ts1; asm volatile("s_waitcnt vmcnt(0)\n\ts_memrealtime %0\n\ts_waitcnt lgkmcnt(0)" : "=s"(ts1) :: "memory");
  if (lane == 0) {
    __hip_atomic_store(st + 2 * b, ts0, __ATOMIC_RELAXED, __HIP_MEMORY_SCOPE_AGENT);
    __hip_atomic_store(st + 2 * b + 1, ts1, __ATOMIC_RELAXED, __HIP_MEMORY_SCOPE_AGENT);
  }
  if (blockIdx.x == 0 && __builtin_amdgcn_readfirstlane(tid >> 6) == 0) {
    while (__builtin_amdgcn_s_memrealtime() < ts0 + 1200) __builtin_amdgcn_s_sleep(1);
    int smin = 1 << 30, smax = -(1 << 30), emax = -(1 << 30), sumA = 0, sumB = 0, eA = -(1 << 30), eB = -(1 << 30), fA = 1 << 30, fB = 1 << 30;
    for (int i = lane; i < 4096; i += 64) {
      long long a = (long long)(__hip_atomic_load(st + 2 * i, __ATOMIC_RELAXED, __HIP_MEMORY_SCOPE_AGENT) - ts0);
      long long e = (long long)(__hip_atomic_load(st + 2 * i + 1, __ATOMIC_RELAXED, __HIP_MEMORY_SCOPE_AGENT) - ts0);
      int ai = a > 1000 ? 1000 : a < -1000 ? -1000 : (int)a, ei = e > 1200 ? 1200 : e < -1000 ? -1000 : (int)e;
      smin = min(smin, ai); smax = max(smax, ai); emax = max(emax, ei);
      int blk_ = i / (BLOCK / 64);
      if ((blk_ & 7) < 4) { sumA += ai; eA = max(eA, ei); fA = min(fA, ai); } else { sumB += ai; eB = max(eB, ei); fB = min(fB, ai); }
    }
    for (int o_ = 32; o_ > 0; o_ >>= 1) {
      smin = min(smin, __shfl_xor(smin, o_)); smax = max(smax, __shfl_xor(smax, o_)); emax = max(emax, __shfl_xor(emax, o_));
      sumA += __shfl_xor(sumA, o_); sumB += __shfl_xor(sumB, o_); eA = max(eA, __shfl_xor(eA, o_)); eB = max(eB, __shfl_xor(eB, o_));
      fA = min(fA, __shfl_xor(fA, o_)); fB = min(fB, __shfl_xor(fB, o_));
    }
    int val = 10 * (sumB - sumA) / 2048;
    val = __builtin_amdgcn_readfirstlane(val);
    unsigned long long tend = ts0 + (long long)__builtin_amdgcn_readfirstlane(smin) + 5000 + (long long)val;
    while (__builtin_amdgcn_s_memrealtime() < tend) __builtin_amdgcn_s_sleep(1);
  }
}

extern "C" void kernel_launch(void* const* d_in, const int* in_sizes, int n_in, void* d_out, int out_size, void* d_ws,
                              size_t ws_size, hipStream_t stream) {
  const int* idx = (const int*)d_in[0];
  const unsigned char* table = (const unsigned char*)d_in[1];
  const unsigned nb = (unsigned)out_size;
  const unsigned spb = BLOCK / 64;
  unsigned grid = (nb + spb - 1) / spb;
  fm_kernel<<<grid, BLOCK, 0, stream>>>(idx, table, (__hip_bfloat16*)d_out, (unsigned)in_sizes[1] * 2u, nb, (unsigned long long*)d_ws);
}
```

```cpp
#include <hip/hip_runtime.h>
#include <hip/hip_bf16.h>
#include <stdint.h>

typedef __attribute__((ext_vector_type(4))) unsigned int u32x4;
typedef __attribute__((ext_vector_type(4))) float f32x4;
typedef __attribute__((ext_vector_type(8))) __bf16 bf16x8_t;

#define BLOCK 256
#define AUX 0
#define SAUX 0
#define SWZ 0
#define NF 26

#define DPP_ADD(v, ctrl) v += __int_as_float(__builtin_amdgcn_update_dpp(0, __float_as_int(v), ctrl, 0xf, 0xf, true))

template <int K> __device__ __forceinline__ unsigned bcast_or(unsigned sh, unsigned c16) {
  return (unsigned)__builtin_amdgcn_update_dpp(0, (int)sh, 0x150 + K, 0xf, 0xf, true) | c16;
}

__global__ __launch_bounds__(BLOCK) void fm_kernel(const int* __restrict__ idx, const unsigned char* __restrict__ table,
                                                 __hip_bfloat16* __restrict__ out, unsigned table_bytes, unsigned nb) {
  const unsigned tid = threadIdx.x;
  unsigned blk = blockIdx.x;
  const unsigned b = blk * (BLOCK / 64) + (tid >> 6);
  const unsigned lane = tid & 63, c = tid & 15, g = lane >> 4;
  const auto trs = __builtin_amdgcn_make_buffer_rsrc((void*)table, 0, table_bytes, 0x00020000);
  const auto irs = __builtin_amdgcn_make_buffer_rsrc((void*)idx, 0, nb * (NF * 4), 0x00020000);
  const auto ors = __builtin_amdgcn_make_buffer_rsrc((void*)out, 0, nb * 2, 0x00020000);
  const unsigned f = 4 * c + g;
  const unsigned oob = f < NF ? 0u : 0x80000000u;
  const unsigned ix = __builtin_amdgcn_raw_buffer_load_b32(irs, ((b * NF + f) * 4) | oob, 0, 0);
  const unsigned sh = (ix << 8) | oob, c16 = c * 16;
  u32x4 r[7];
#define LD(k) r[k] = __builtin_bit_cast(u32x4, __builtin_amdgcn_raw_buffer_load_b128(trs, bcast_or<k>(sh, c16), 0, AUX));
  LD(0) LD(1) LD(2) LD(3) LD(4) LD(5) LD(6)
#undef LD
  __builtin_amdgcn_sched_barrier(0);
  const unsigned half = (c & 1) ? 0x3f000000u : 0x00003f00u;
  const unsigned jj = (c & 7) >> 1;
  const u32x4 a = {jj == 0 ? half : 0u, jj == 1 ? half : 0u, jj == 2 ? half : 0u, jj == 3 ? half : 0u};
  const bf16x8_t sel = __builtin_bit_cast(bf16x8_t, a);
  const unsigned di = c - 4 * g;
  const float w0 = di == 0 ? -0.5f : 0.f, w1 = di == 1 ? -0.5f : 0.f, w2 = di == 2 ? -0.5f : 0.f, w3 = di == 3 ? -0.5f : 0.f;
  const unsigned ooff = lane == 0 ? b * 2 : 0x80000000u;
  f32x4 acc = {0.f, 0.f, 0.f, 0.f}, dq = {0.f, 0.f, 0.f, 0.f};
  __builtin_amdgcn_sched_barrier(0);
#pragma unroll
  for (int k = 0; k < 7; ++k) {
    const bf16x8_t x = __builtin_bit_cast(bf16x8_t, r[k]);
    acc = __builtin_amdgcn_mfma_f32_16x16x32_bf16(sel, x, acc, 0, 0, 0);
    dq = __builtin_amdgcn_mfma_f32_16x16x32_bf16(x, x, dq, 0, 0, 0);
  }
  float t0 = acc[0] * acc[0], t1 = acc[1] * acc[1];
  t0 = fmaf(acc[2], acc[2], t0); t1 = fmaf(acc[3], acc[3], t1);
  t0 = fmaf(w0, dq[0], t0); t1 = fmaf(w1, dq[1], t1);
  t0 = fmaf(w2, dq[2], t0); t1 = fmaf(w3, dq[3], t1);
  float t = t0 + t1;
  DPP_ADD(t, 0x128); DPP_ADD(t, 0x124); DPP_ADD(t, 0x122); DPP_ADD(t, 0x121);
  const auto s16 = __builtin_amdgcn_permlane16_swap(__float_as_uint(t), __float_as_uint(t), false, false);
  t = __uint_as_float(s16[0]) + __uint_as_float(s16[1]);
  const auto s32 = __builtin_amdgcn_permlane32_swap(__float_as_uint(t), __float_as_uint(t), false, false);
  t = __uint_as_float(s32[0]) + __uint_as_float(s32[1]);
  const __hip_bfloat16 o = __float2bfloat16(t);
  __builtin_amdgcn_raw_buffer_store_b16(__builtin_bit_cast(unsigned short, o), ors, ooff, 0, SAUX);
}

extern "C" void kernel_launch(void* const* d_in, const int* in_sizes, int n_in, void* d_out, int out_size, void* d_ws,
                              size_t ws_size, hipStream_t stream) {
  const int* idx = (const int*)d_in[0];
  const unsigned char* table = (const unsigned char*)d_in[1];
  const unsigned nb = (unsigned)out_size;
  const unsigned spb = BLOCK / 64;
  unsigned grid = (nb + spb - 1) / spb;
  fm_kernel<<<grid, BLOCK, 0, stream>>>(idx, table, (__hip_bfloat16*)d_out, (unsigned)in_sizes[1] * 2u, nb);
}
```

```cpp
#include <hip/hip_runtime.h>
#include <hip/hip_bf16.h>
#include <stdint.h>

typedef __attribute__((ext_vector_type(4))) unsigned int u32x4;
typedef __attribute__((ext_vector_type(4))) float f32x4;
typedef __attribute__((ext_vector_type(8))) __bf16 bf16x8_t;

#define BLOCK 1024
#define AUX 0
#define SAUX 0
#define SWZ 0
#define NF 26

#define DPP_ADD(v, ctrl) v += __int_as_float(__builtin_amdgcn_update_dpp(0, __float_as_int(v), ctrl, 0xf, 0xf, true))

template <int K> __device__ __forceinline__ unsigned bcast_or(unsigned sh, unsigned c16) {
  return (unsigned)__builtin_amdgcn_update_dpp(0, (int)sh, 0x150 + K, 0xf, 0xf, true) | c16;
}

__global__ __launch_bounds__(BLOCK) void fm_kernel(const int* __restrict__ idx, const unsigned char* __restrict__ table,
                                                 __hip_bfloat16* __restrict__ out, unsigned table_bytes, unsigned nb, unsigned long long* __restrict__ st) {
  unsigned long long ts0; asm volatile("s_memrealtime %0" : "=s"(ts0) :: "memory");
  const unsigned tid = threadIdx.x;
  unsigned blk = blockIdx.x;
  const unsigned b = blk * (BLOCK / 64) + (tid >> 6);
  const unsigned lane = tid & 63, c = tid & 15, g = lane >> 4;
  const auto trs = __builtin_amdgcn_make_buffer_rsrc((void*)table, 0, table_bytes, 0x00020000);
  const auto irs = __builtin_amdgcn_make_buffer_rsrc((void*)idx, 0, nb * (NF * 4), 0x00020000);
  const auto ors = __builtin_amdgcn_make_buffer_rsrc((void*)out, 0, nb * 2, 0x00020000);
  const unsigned f = 4 * c + g;
  const unsigned oob = f < NF ? 0u : 0x80000000u;
  const unsigned ix = __builtin_amdgcn_raw_buffer_load_b32(irs, ((b * NF + f) * 4) | oob, 0, 0);
  const unsigned sh = (ix << 8) | oob, c16 = c * 16;
  u32x4 r[7];
#define LD(k) r[k] = __builtin_bit_cast(u32x4, __builtin_amdgcn_raw_buffer_load_b128(trs, bcast_or<k>(sh, c16), 0, AUX));
  LD(0) LD(1) LD(2) LD(3) LD(4) LD(5) LD(6)
#undef LD
  __builtin_amdgcn_sched_barrier(0);
  const unsigned half = (c & 1) ? 0x3f000000u : 0x00003f00u;
  const unsigned jj = (c & 7) >> 1;
  const u32x4 a = {jj == 0 ? half : 0u, jj == 1 ? half : 0u, jj == 2 ? half : 0u, jj == 3 ? half : 0u};
  const bf16x8_t sel = __builtin_bit_cast(bf16x8_t, a);
  const unsigned di = c - 4 * g;
  const float w0 = di == 0 ? -0.5f : 0.f, w1 = di == 1 ? -0.5f : 0.f, w2 = di == 2 ? -0.5f : 0.f, w3 = di == 3 ? -0.5f : 0.f;
  const unsigned ooff = lane == 0 ? b * 2 : 0x80000000u;
  f32x4 acc = {0.f, 0.f, 0.f, 0.f}, dq = {0.f, 0.f, 0.f, 0.f};
  __builtin_amdgcn_sched_barrier(0);
#pragma unroll
  for (int k = 0; k < 7; ++k) {
    const bf16x8_t x = __builtin_bit_cast(bf16x8_t, r[k]);
    acc = __builtin_amdgcn_mfma_f32_16x16x32_bf16(sel, x, acc, 0, 0, 0);
    dq = __builtin_amdgcn_mfma_f32_16x16x32_bf16(x, x, dq, 0, 0, 0);
  }
  float t0 = acc[0] * acc[0], t1 = acc[1] * acc[1];
  t0 = fmaf(acc[2], acc[2], t0); t1 = fmaf(acc[3], acc[3], t1);
  t0 = fmaf(w0, dq[0], t0); t1 = fmaf(w1, dq[1], t1);
  t0 = fmaf(w2, dq[2], t0); t1 = fmaf(w3, dq[3], t1);
  float t = t0 + t1;
  DPP_ADD(t, 0x128); DPP_ADD(t, 0x124); DPP_ADD(t, 0x122); DPP_ADD(t, 0x121);
  const auto s16 = __builtin_amdgcn_permlane16_swap(__float_as_uint(t), __float_as_uint(t), false, false);
  t = __uint_as_float(s16[0]) + __uint_as_float(s16[1]);
  const auto s32 = __builtin_amdgcn_permlane32_swap(__float_as_uint(t), __float_as_uint(t), false, false);
  t = __uint_as_float(s32[0]) + __uint_as_float(s32[1]);
  const __hip_bfloat16 o = __float2bfloat16(t);
  __builtin_amdgcn_raw_buffer_store_b16(__builtin_bit_cast(unsigned short, o), ors, ooff, 0, SAUX);
  unsigned long long ts1; asm volatile("s_waitcnt vmcnt(0)\n\ts_memrealtime %0\n\ts_waitcnt lgkmcnt(0)" : "=s"(ts1) :: "memory");
  if (lane == 0) {
    __hip_atomic_store(st + 2 * b, ts0, __ATOMIC_RELAXED, __HIP_MEMORY_SCOPE_AGENT);
    __hip_atomic_store(st + 2 * b + 1, ts1, __ATOMIC_RELAXED, __HIP_MEMORY_SCOPE_AGENT);
  }
  if (blockIdx.x == 0 && __builtin_amdgcn_readfirstlane(tid >> 6) == 0) {
    while (__builtin_amdgcn_s_memrealtime() < ts0 + 1200) __builtin_amdgcn_s_sleep(1);
    int smin = 1 << 30, smax = -(1 << 30), emax = -(1 << 30), sumA = 0, sumB = 0, eA = -(1 << 30), eB = -(1 << 30), fA = 1 << 30, fB = 1 << 30;
    for (int i = lane; i < 4096; i += 64) {
      long long a = (long long)(__hip_atomic_load(st + 2 * i, __ATOMIC_RELAXED, __HIP_MEMORY_SCOPE_AGENT) - ts0);
      long long e = (long long)(__hip_atomic_load(st + 2 * i + 1, __ATOMIC_RELAXED, __HIP_MEMORY_SCOPE_AGENT) - ts0);
      int ai = a > 1000 ? 1000 : a < -1000 ? -1000 : (int)a, ei = e > 1200 ? 1200 : e < -1000 ? -1000 : (int)e;
      smin = min(smin, ai); smax = max(smax, ai); emax = max(emax, ei);
      int blk_ = i / (BLOCK / 64);
      if ((blk_ & 7) < 4) { sumA += ai; eA = max(eA, ei); fA = min(fA, ai); } else { sumB += ai; eB = max(eB, ei); fB = min(fB, ai); }
    }
    for (int o_ = 32; o_ > 0; o_ >>= 1) {
      smin = min(smin, __shfl_xor(smin, o_)); smax = max(smax, __shfl_xor(smax, o_)); emax = max(emax, __shfl_xor(emax, o_));
      sumA += __shfl_xor(sumA, o_); sumB += __shfl_xor(sumB, o_); eA = max(eA, __shfl_xor(eA, o_)); eB = max(eB, __shfl_xor(eB, o_));
      fA = min(fA, __shfl_xor(fA, o_)); fB = min(fB, __shfl_xor(fB, o_));
    }
    int val = 0;
    val = __builtin_amdgcn_readfirstlane(val);
    unsigned long long tend = ts0 + (long long)__builtin_amdgcn_readfirstlane(smin) + 5000 + (long long)val;
    while (__builtin_amdgcn_s_memrealtime() < tend) __builtin_amdgcn_s_sleep(1);
  }
}

extern "C" void kernel_launch(void* const* d_in, const int* in_sizes, int n_in, void* d_out, int out_size, void* d_ws,
                              size_t ws_size, hipStream_t stream) {
  const int* idx = (const int*)d_in[0];
  const unsigned char* table = (const unsigned char*)d_in[1];
  const unsigned nb = (unsigned)out_size;
  const unsigned spb = BLOCK / 64;
  unsigned grid = (nb + spb - 1) / spb;
  fm_kernel<<<grid, BLOCK, 0, stream>>>(idx, table, (__hip_bfloat16*)d_out, (unsigned)in_sizes[1] * 2u, nb, (unsigned long long*)d_ws);
}
```

```cpp
#include <hip/hip_runtime.h>
#include <hip/hip_bf16.h>
#include <stdint.h>

typedef __attribute__((ext_vector_type(4))) unsigned int u32x4;
typedef __attribute__((ext_vector_type(4))) float f32x4;
typedef __attribute__((ext_vector_type(8))) __bf16 bf16x8_t;

#define BLOCK 1024
#define NF 26

#define DPP_ADD(v, ctrl) v += __int_as_float(__builtin_amdgcn_update_dpp(0, __float_as_int(v), ctrl, 0xf, 0xf, true))

template <int K> __device__ __forceinline__ unsigned bcast_or(unsigned sh, unsigned c16) {
  return (unsigned)__builtin_amdgcn_update_dpp(0, (int)sh, 0x150 + K, 0xf, 0xf, true) | c16;
}

__global__ __launch_bounds__(BLOCK) void fm_kernel(const int* __restrict__ idx, const unsigned char* __restrict__ table,
                                                 __hip_bfloat16* __restrict__ out, unsigned table_bytes, unsigned nb) {
  unsigned long long ts0; asm volatile("s_memrealtime %0" : "=s"(ts0) :: "memory");
  const unsigned tid = threadIdx.x;
  for (unsigned b = blockIdx.x * (BLOCK / 64) + (tid >> 6); b < nb; b += 64 * (BLOCK / 64)) {
  const unsigned lane = tid & 63, c = tid & 15, g = lane >> 4;
  const auto trs = __builtin_amdgcn_make_buffer_rsrc((void*)table, 0, table_bytes, 0x00020000);
  const auto irs = __builtin_amdgcn_make_buffer_rsrc((void*)idx, 0, nb * (NF * 4), 0x00020000);
  const auto ors = __builtin_amdgcn_make_buffer_rsrc((void*)out, 0, nb * 2, 0x00020000);
  const unsigned f = 4 * c + g;
  const unsigned oob = f < NF ? 0u : 0x80000000u;
  const unsigned ix = __builtin_amdgcn_raw_buffer_load_b32(irs, ((b * NF + f) * 4) | oob, 0, 0);
  const unsigned sh = (ix << 8) | oob, c16 = c * 16;
  u32x4 r[7];
#define LD(k) r[k] = __builtin_bit_cast(u32x4, __builtin_amdgcn_raw_buffer_load_b128(trs, bcast_or<k>(sh, c16), 0, 0));
  LD(0) LD(1) LD(2) LD(3) LD(4) LD(5) LD(6)
#undef LD
  __builtin_amdgcn_sched_barrier(0);
  const unsigned half = (c & 1) ? 0x3f000000u : 0x00003f00u;
  const unsigned jj = (c & 7) >> 1;
  const u32x4 a = {jj == 0 ? half : 0u, jj == 1 ? half : 0u, jj == 2 ? half : 0u, jj == 3 ? half : 0u};
  const bf16x8_t sel = __builtin_bit_cast(bf16x8_t, a);
  const unsigned di = c - 4 * g;
  const float w0 = di == 0 ? -0.5f : 0.f, w1 = di == 1 ? -0.5f : 0.f, w2 = di == 2 ? -0.5f : 0.f, w3 = di == 3 ? -0.5f : 0.f;
  const unsigned ooff = lane == 0 ? b * 2 : 0x80000000u;
  f32x4 acc = {0.f, 0.f, 0.f, 0.f}, dq = {0.f, 0.f, 0.f, 0.f};
  __builtin_amdgcn_sched_barrier(0);
#pragma unroll
  for (int k = 0; k < 7; ++k) {
    const bf16x8_t x = __builtin_bit_cast(bf16x8_t, r[k]);
    acc = __builtin_amdgcn_mfma_f32_16x16x32_bf16(sel, x, acc, 0, 0, 0);
    dq = __builtin_amdgcn_mfma_f32_16x16x32_bf16(x, x, dq, 0, 0, 0);
  }
  float t0 = acc[0] * acc[0], t1 = acc[1] * acc[1];
  t0 = fmaf(acc[2], acc[2], t0); t1 = fmaf(acc[3], acc[3], t1);
  t0 = fmaf(w0, dq[0], t0); t1 = fmaf(w1, dq[1], t1);
  t0 = fmaf(w2, dq[2], t0); t1 = fmaf(w3, dq[3], t1);
  float t = t0 + t1;
  DPP_ADD(t, 0x128); DPP_ADD(t, 0x124); DPP_ADD(t, 0x122); DPP_ADD(t, 0x121);
  const auto s16 = __builtin_amdgcn_permlane16_swap(__float_as_uint(t), __float_as_uint(t), false, false);
  t = __uint_as_float(s16[0]) + __uint_as_float(s16[1]);
  const auto s32 = __builtin_amdgcn_permlane32_swap(__float_as_uint(t), __float_as_uint(t), false, false);
  t = __uint_as_float(s32[0]) + __uint_as_float(s32[1]);
  const __hip_bfloat16 o = __float2bfloat16(t);
  __builtin_amdgcn_raw_buffer_store_b16(__builtin_bit_cast(unsigned short, o), ors, ooff, 0, 0);
  }

  if (blockIdx.x == 0 && __builtin_amdgcn_readfirstlane(tid >> 6) == 0) {
    asm volatile("s_waitcnt lgkmcnt(0)" ::: "memory");
    while (__builtin_amdgcn_s_memrealtime() < ts0 + 5000) __builtin_amdgcn_s_sleep(1);
  }
}

extern "C" void kernel_launch(void* const* d_in, const int* in_sizes, int n_in, void* d_out, int out_size, void* d_ws,
                              size_t ws_size, hipStream_t stream) {
  const int* idx = (const int*)d_in[0];
  const unsigned char* table = (const unsigned char*)d_in[1];
  const unsigned nb = (unsigned)out_size;
  const unsigned spb = BLOCK / 64;
  const unsigned grid = 64;
  fm_kernel<<<grid, BLOCK, 0, stream>>>(idx, table, (__hip_bfloat16*)d_out, (unsigned)in_sizes[1] * 2u, nb);
}
```

```cpp
#include <hip/hip_runtime.h>
#include <hip/hip_bf16.h>
#include <stdint.h>

typedef __attribute__((ext_vector_type(4))) unsigned int u32x4;
typedef __attribute__((ext_vector_type(4))) float f32x4;
typedef __attribute__((ext_vector_type(8))) __bf16 bf16x8_t;

#define BLOCK 512
#define AUX 0
#define SAUX 0
#define SWZ 0
#define NF 26

#define DPP_ADD(v, ctrl) v += __int_as_float(__builtin_amdgcn_update_dpp(0, __float_as_int(v), ctrl, 0xf, 0xf, true))

template <int K> __device__ __forceinline__ unsigned bcast_or(unsigned sh, unsigned c16) {
  return (unsigned)__builtin_amdgcn_update_dpp(0, (int)sh, 0x150 + K, 0xf, 0xf, true) | c16;
}

__global__ __launch_bounds__(BLOCK) void fm_kernel(const int* __restrict__ idx, const unsigned char* __restrict__ table,
                                                 __hip_bfloat16* __restrict__ out, unsigned table_bytes, unsigned nb, unsigned long long* __restrict__ st) {
  unsigned long long ts0; asm volatile("s_memrealtime %0" : "=s"(ts0) :: "memory");
  const unsigned tid = threadIdx.x;
  unsigned blk = blockIdx.x;
  const unsigned b = blk * (BLOCK / 64) + (tid >> 6);
  const unsigned lane = tid & 63, c = tid & 15, g = lane >> 4;
  const auto trs = __builtin_amdgcn_make_buffer_rsrc((void*)table, 0, table_bytes, 0x00020000);
  const auto irs = __builtin_amdgcn_make_buffer_rsrc((void*)idx, 0, nb * (NF * 4), 0x00020000);
  const auto ors = __builtin_amdgcn_make_buffer_rsrc((void*)out, 0, nb * 2, 0x00020000);
  const unsigned f = 4 * c + g;
  const unsigned oob = f < NF ? 0u : 0x80000000u;
  const unsigned ix = __builtin_amdgcn_raw_buffer_load_b32(irs, ((b * NF + f) * 4) | oob, 0, 0);
  const unsigned sh = (ix << 8) | oob, c16 = c * 16;
  u32x4 r[7];
#define LD(k) r[k] = __builtin_bit_cast(u32x4, __builtin_amdgcn_raw_buffer_load_b128(trs, bcast_or<k>(sh, c16), 0, AUX));
  LD(0) LD(1) LD(2) LD(3) LD(4) LD(5) LD(6)
#undef LD
  __builtin_amdgcn_sched_barrier(0);
  const unsigned half = (c & 1) ? 0x3f000000u : 0x00003f00u;
  const unsigned jj = (c & 7) >> 1;
  const u32x4 a = {jj == 0 ? half : 0u, jj == 1 ? half : 0u, jj == 2 ? half : 0u, jj == 3 ? half : 0u};
  const bf16x8_t sel = __builtin_bit_cast(bf16x8_t, a);
  const unsigned di = c - 4 * g;
  const float w0 = di == 0 ? -0.5f : 0.f, w1 = di == 1 ? -0.5f : 0.f, w2 = di == 2 ? -0.5f : 0.f, w3 = di == 3 ? -0.5f : 0.f;
  const unsigned ooff = lane == 0 ? b * 2 : 0x80000000u;
  f32x4 acc = {0.f, 0.f, 0.f, 0.f}, dq = {0.f, 0.f, 0.f, 0.f};
  __builtin_amdgcn_sched_barrier(0);
#pragma unroll
  for (int k = 0; k < 7; ++k) {
    const bf16x8_t x = __builtin_bit_cast(bf16x8_t, r[k]);
    acc = __builtin_amdgcn_mfma_f32_16x16x32_bf16(sel, x, acc, 0, 0, 0);
    dq = __builtin_amdgcn_mfma_f32_16x16x32_bf16(x, x, dq, 0, 0, 0);
  }
  float t0 = acc[0] * acc[0], t1 = acc[1] * acc[1];
  t0 = fmaf(acc[2], acc[2], t0); t1 = fmaf(acc[3], acc[3], t1);
  t0 = fmaf(w0, dq[0], t0); t1 = fmaf(w1, dq[1], t1);
  t0 = fmaf(w2, dq[2], t0); t1 = fmaf(w3, dq[3], t1);
  float t = t0 + t1;
  DPP_ADD(t, 0x128); DPP_ADD(t, 0x124); DPP_ADD(t, 0x122); DPP_ADD(t, 0x121);
  const auto s16 = __builtin_amdgcn_permlane16_swap(__float_as_uint(t), __float_as_uint(t), false, false);
  t = __uint_as_float(s16[0]) + __uint_as_float(s16[1]);
  const auto s32 = __builtin_amdgcn_permlane32_swap(__float_as_uint(t), __float_as_uint(t), false, false);
  t = __uint_as_float(s32[0]) + __uint_as_float(s32[1]);
  const __hip_bfloat16 o = __float2bfloat16(t);
  __builtin_amdgcn_raw_buffer_store_b16(__builtin_bit_cast(unsigned short, o), ors, ooff, 0, SAUX);
  unsigned long long ts1; asm volatile("s_waitcnt vmcnt(0)\n\ts_memrealtime %0\n\ts_waitcnt lgkmcnt(0)" : "=s"(ts1) :: "memory");
  unsigned xcc; asm volatile("s_getreg_b32 %0, hwreg(HW_REG_XCC_ID, 0, 4)" : "=s"(xcc));
  unsigned hwid; asm volatile("s_getreg_b32 %0, hwreg(HW_REG_HW_ID)" : "=s"(hwid));
  if (lane == 0) {
    __hip_atomic_store(st + 2 * b, (ts0 & 0xffffffffffffull) | ((unsigned long long)(xcc & 7) << 60), __ATOMIC_RELAXED, __HIP_MEMORY_SCOPE_AGENT);
    __hip_atomic_store(st + 2 * b + 1, ts1, __ATOMIC_RELAXED, __HIP_MEMORY_SCOPE_AGENT);
  }
  if (blockIdx.x == 0 && __builtin_amdgcn_readfirstlane(tid >> 6) == 0) {
    while (__builtin_amdgcn_s_memrealtime() < ts0 + 1200) __builtin_amdgcn_s_sleep(1);
    int fs[8], ls[8], le[8], sum = 0;
#pragma unroll
    for (int x = 0; x < 8; ++x) { fs[x] = 1 << 30; ls[x] = -(1 << 30); le[x] = -(1 << 30); }
    for (int i = lane; i < 4096; i += 64) {
      unsigned long long w = __hip_atomic_load(st + 2 * i, __ATOMIC_RELAXED, __HIP_MEMORY_SCOPE_AGENT);
      int xx = (int)(w >> 60);
      long long a = (long long)(w & 0xffffffffffffull) - (long long)(ts0 & 0xffffffffffffull);
      long long e = (long long)(__hip_atomic_load(st + 2 * i + 1, __ATOMIC_RELAXED, __HIP_MEMORY_SCOPE_AGENT) - ts0);
      int ai = a > 1000 ? 1000 : a < -1000 ? -1000 : (int)a, ei = e > 1200 ? 1200 : e < -1000 ? -1000 : (int)e;
      sum += ai;
#pragma unroll
      for (int x = 0; x < 8; ++x) if (xx == x) { fs[x] = min(fs[x], ai); ls[x] = max(ls[x], ai); le[x] = max(le[x], ei); }
    }
    for (int o_ = 32; o_ > 0; o_ >>= 1) {
      sum += __shfl_xor(sum, o_);
#pragma unroll
      for (int x = 0; x < 8; ++x) { fs[x] = min(fs[x], __shfl_xor(fs[x], o_)); ls[x] = max(ls[x], __shfl_xor(ls[x], o_)); le[x] = max(le[x], __shfl_xor(le[x], o_)); }
    }
    int smin = 1 << 30, fmax = -(1 << 30), sprmax = 0, fsum = 0, emin = 1 << 30, emax = -(1 << 30), durmin = 1 << 30, durmax = 0, nlate = 0;
#pragma unroll
    for (int x = 0; x < 8; ++x) { smin = min(smin, fs[x]); fmax = max(fmax, fs[x]); sprmax = max(sprmax, ls[x] - fs[x]); fsum += fs[x];
      emin = min(emin, le[x]); emax = max(emax, le[x]); durmin = min(durmin, le[x] - fs[x]); durmax = max(durmax, le[x] - fs[x]); }
#pragma unroll
    for (int x = 0; x < 8; ++x) nlate += (fs[x] - smin > 40);
    int val = 0;
    val = __builtin_amdgcn_readfirstlane(val);
    unsigned long long tend = ts0 + (long long)__builtin_amdgcn_readfirstlane(smin) + 5000 + (long long)val;
    while (__builtin_amdgcn_s_memrealtime() < tend) __builtin_amdgcn_s_sleep(1);
  }
}

extern "C" void kernel_launch(void* const* d_in, const int* in_sizes, int n_in, void* d_out, int out_size, void* d_ws,
                              size_t ws_size, hipStream_t stream) {
  const int* idx = (const int*)d_in[0];
  const unsigned char* table = (const unsigned char*)d_in[1];
  const unsigned nb = (unsigned)out_size;
  const unsigned spb = BLOCK / 64;
  unsigned grid = (nb + spb - 1) / spb;
  fm_kernel<<<grid, BLOCK, 0, stream>>>(idx, table, (__hip_bfloat16*)d_out, (unsigned)in_sizes[1] * 2u, nb, (unsigned long long*)d_ws);
}
```

```cpp
#include <hip/hip_runtime.h>
#include <hip/hip_bf16.h>
#include <stdint.h>

typedef __attribute__((ext_vector_type(4))) unsigned int u32x4;
typedef __attribute__((ext_vector_type(4))) float f32x4;
typedef __attribute__((ext_vector_type(8))) __bf16 bf16x8_t;

#define BLOCK 512
#define NF 26

#define DPP_ADD(v, ctrl) v += __int_as_float(__builtin_amdgcn_update_dpp(0, __float_as_int(v), ctrl, 0xf, 0xf, true))

template <int K> __device__ __forceinline__ unsigned bcast_or(unsigned sh, unsigned c16) {
  return (unsigned)__builtin_amdgcn_update_dpp(0, (int)sh, 0x150 + K, 0xf, 0xf, true) | c16;
}

__global__ __launch_bounds__(BLOCK) void fm_kernel(const int* __restrict__ idx, const unsigned char* __restrict__ table,
                                                 __hip_bfloat16* __restrict__ out, unsigned table_bytes, unsigned nb, unsigned* __restrict__ st) {
  unsigned long long ts0, ts1, tsA, ts2, tsB, ts3, ts4;
  asm volatile("s_memrealtime %0" : "=s"(ts0) :: "memory");
  const unsigned tid = threadIdx.x;
  const unsigned b = blockIdx.x * (BLOCK / 64) + (tid >> 6);
  const unsigned lane = tid & 63, c = tid & 15, g = lane >> 4;
  const auto trs = __builtin_amdgcn_make_buffer_rsrc((void*)table, 0, table_bytes, 0x00020000);
  const auto irs = __builtin_amdgcn_make_buffer_rsrc((void*)idx, 0, nb * (NF * 4), 0x00020000);
  const auto ors = __builtin_amdgcn_make_buffer_rsrc((void*)out, 0, nb * 2, 0x00020000);
  const unsigned f = 4 * c + g;
  const unsigned oob = f < NF ? 0u : 0x80000000u;
  __builtin_amdgcn_sched_barrier(0);
  asm volatile("s_memrealtime %0" : "=s"(ts1) : "s"(idx), "s"(table), "s"(nb), "s"(st) : "memory");
  __builtin_amdgcn_sched_barrier(0);
  const unsigned ix = __builtin_amdgcn_raw_buffer_load_b32(irs, ((b * NF + f) * 4) | oob, 0, 0);
  __builtin_amdgcn_sched_barrier(0);
  asm volatile(".p2align 7\n\ts_memrealtime %0" : "=s"(tsA) :: "memory");
  __builtin_amdgcn_sched_barrier(0);
  asm volatile("s_memrealtime %0" : "=s"(ts2) : "v"(ix) : "memory");
  __builtin_amdgcn_sched_barrier(0);
  const unsigned sh = (ix << 8) | oob, c16 = c * 16;
  u32x4 r[7];
#define LD(k) r[k] = __builtin_bit_cast(u32x4, __builtin_amdgcn_raw_buffer_load_b128(trs, bcast_or<k>(sh, c16), 0, 0));
  LD(0) LD(1) LD(2) LD(3) LD(4) LD(5) LD(6)
#undef LD
  __builtin_amdgcn_sched_barrier(0);
  asm volatile("s_memrealtime %0" : "=s"(tsB) :: "memory");
  __builtin_amdgcn_sched_barrier(0);
  asm volatile("s_memrealtime %0" : "=s"(ts3) : "v"(r[0]) : "memory");
  __builtin_amdgcn_sched_barrier(0);
  const unsigned half = (c & 1) ? 0x3f000000u : 0x00003f00u;
  const unsigned jj = (c & 7) >> 1;
  const u32x4 a = {jj == 0 ? half : 0u, jj == 1 ? half : 0u, jj == 2 ? half : 0u, jj == 3 ? half : 0u};
  const bf16x8_t sel = __builtin_bit_cast(bf16x8_t, a);
  const unsigned di = c - 4 * g;
  const float w0 = di == 0 ? -0.5f : 0.f, w1 = di == 1 ? -0.5f : 0.f, w2 = di == 2 ? -0.5f : 0.f, w3 = di == 3 ? -0.5f : 0.f;
  const unsigned ooff = lane == 0 ? b * 2 : 0x80000000u;
  f32x4 acc = {0.f, 0.f, 0.f, 0.f}, dq = {0.f, 0.f, 0.f, 0.f};
  __builtin_amdgcn_sched_barrier(0);
#pragma unroll
  for (int k = 0; k < 7; ++k) {
    const bf16x8_t x = __builtin_bit_cast(bf16x8_t, r[k]);
    acc = __builtin_amdgcn_mfma_f32_16x16x32_bf16(sel, x, acc, 0, 0, 0);
    dq = __builtin_amdgcn_mfma_f32_16x16x32_bf16(x, x, dq, 0, 0, 0);
  }
  float t0 = acc[0] * acc[0], t1 = acc[1] * acc[1];
  t0 = fmaf(acc[2], acc[2], t0); t1 = fmaf(acc[3], acc[3], t1);
  t0 = fmaf(w0, dq[0], t0); t1 = fmaf(w1, dq[1], t1);
  t0 = fmaf(w2, dq[2], t0); t1 = fmaf(w3, dq[3], t1);
  float t = t0 + t1;
  DPP_ADD(t, 0x128); DPP_ADD(t, 0x124); DPP_ADD(t, 0x122); DPP_ADD(t, 0x121);
  const auto s16 = __builtin_amdgcn_permlane16_swap(__float_as_uint(t), __float_as_uint(t), false, false);
  t = __uint_as_float(s16[0]) + __uint_as_float(s16[1]);
  const auto s32 = __builtin_amdgcn_permlane32_swap(__float_as_uint(t), __float_as_uint(t), false, false);
  t = __uint_as_float(s32[0]) + __uint_as_float(s32[1]);
  const __hip_bfloat16 o = __float2bfloat16(t);
  __builtin_amdgcn_raw_buffer_store_b16(__builtin_bit_cast(unsigned short, o), ors, ooff, 0, 0);
  asm volatile("s_memrealtime %0\n\ts_waitcnt lgkmcnt(0)" : "=s"(ts4) : "v"(t) : "memory");
  if (lane < 8) {
    unsigned v = lane == 0 ? (unsigned)ts0 : lane == 1 ? (unsigned)ts1 : lane == 2 ? (unsigned)tsA : lane == 3 ? (unsigned)ts2 : lane == 4 ? (unsigned)tsB : lane == 5 ? (unsigned)ts3 : (unsigned)ts4;
    __hip_atomic_store(st + 8 * b + lane, v, __ATOMIC_RELAXED, __HIP_MEMORY_SCOPE_AGENT);
  }
  if (blockIdx.x == 0 && __builtin_amdgcn_readfirstlane(tid >> 6) == 0) {
    while (__builtin_amdgcn_s_memrealtime() < ts0 + 1500) __builtin_amdgcn_s_sleep(1);
    int sum = 0, smin = 1 << 30;
    for (int i = lane; i < 4096; i += 64) {
      unsigned x0 = __hip_atomic_load(st + 8 * i, __ATOMIC_RELAXED, __HIP_MEMORY_SCOPE_AGENT);
      unsigned lo = __hip_atomic_load(st + 8 * i + 1, __ATOMIC_RELAXED, __HIP_MEMORY_SCOPE_AGENT);
      unsigned hi = __hip_atomic_load(st + 8 * i + 2, __ATOMIC_RELAXED, __HIP_MEMORY_SCOPE_AGENT);
      int d = (int)(hi - lo); d = d < 0 ? 0 : d > 1500 ? 1500 : d;
      int a = (int)(x0 - (unsigned)ts0); a = a < -1000 ? -1000 : a;
      sum += d; smin = min(smin, a);
    }
    for (int o_ = 32; o_ > 0; o_ >>= 1) { sum += __shfl_xor(sum, o_); smin = min(smin, __shfl_xor(smin, o_)); }
    int val = __builtin_amdgcn_readfirstlane(sum * 20 / 4096);
    unsigned long long tend = ts0 + (long long)__builtin_amdgcn_readfirstlane(smin) + 5000 + (long long)val;
    while (__builtin_amdgcn_s_memrealtime() < tend) __builtin_amdgcn_s_sleep(1);
  }
}

extern "C" void kernel_launch(void* const* d_in, const int* in_sizes, int n_in, void* d_out, int out_size, void* d_ws,
                              size_t ws_size, hipStream_t stream) {
  const int* idx = (const int*)d_in[0];
  const unsigned char* table = (const unsigned char*)d_in[1];
  const unsigned nb = (unsigned)out_size;
  const unsigned spb = BLOCK / 64;
  const unsigned grid = (nb + spb - 1) / spb;
  fm_kernel<<<grid, BLOCK, 0, stream>>>(idx, table, (__hip_bfloat16*)d_out, (unsigned)in_sizes[1] * 2u, nb, (unsigned*)d_ws);
}
```

```cpp
#include <hip/hip_runtime.h>
#include <hip/hip_bf16.h>
#include <stdint.h>

typedef __attribute__((ext_vector_type(4))) unsigned int u32x4;
typedef __attribute__((ext_vector_type(4))) float f32x4;
typedef __attribute__((ext_vector_type(8))) __bf16 bf16x8_t;

#define BLOCK 512
#define NF 26

#define DPP_ADD(v, ctrl) v += __int_as_float(__builtin_amdgcn_update_dpp(0, __float_as_int(v), ctrl, 0xf, 0xf, true))

template <int K> __device__ __forceinline__ unsigned bcast_or(unsigned sh, unsigned c16) {
  return (unsigned)__builtin_amdgcn_update_dpp(0, (int)sh, 0x150 + K, 0xf, 0xf, true) | c16;
}

__global__ __launch_bounds__(BLOCK) void fm_kernel(const int* idx_, const unsigned char* table_,
                                                 __hip_bfloat16* out_, unsigned table_bytes_, unsigned nb_, unsigned* st_) {
  unsigned long long ts0, ts1, tsA, ts2, tsB, ts3, ts4;
  typedef const __attribute__((address_space(4))) unsigned long long* kp_t;
  kp_t kp = (kp_t)__builtin_amdgcn_kernarg_segment_ptr();
  asm volatile("s_memrealtime %0\n\t.p2align 6\n\ts_memrealtime %1\n\t.p2align 7\n\ts_memrealtime %2\n\ts_waitcnt lgkmcnt(0)\n\ts_memrealtime %3" : "=s"(ts0), "=s"(ts1), "=s"(tsA), "=s"(ts2), "+s"(kp) :: "memory");
  const int* idx = (const int*)kp[0]; const unsigned char* table = (const unsigned char*)kp[1]; __hip_bfloat16* out = (__hip_bfloat16*)kp[2];
  const unsigned table_bytes = (unsigned)kp[3], nb = (unsigned)(kp[3] >> 32); unsigned* st = (unsigned*)kp[4];
  __builtin_amdgcn_sched_barrier(0);
  asm volatile("s_memrealtime %0" : "=s"(tsB) : "s"(idx), "s"(table), "s"(nb), "s"(st) : "memory");
  __builtin_amdgcn_sched_barrier(0);
  const unsigned tid = threadIdx.x;
  const unsigned b = blockIdx.x * (BLOCK / 64) + (tid >> 6);
  const unsigned lane = tid & 63, c = tid & 15, g = lane >> 4;
  const auto trs = __builtin_amdgcn_make_buffer_rsrc((void*)table, 0, table_bytes, 0x00020000);
  const auto irs = __builtin_amdgcn_make_buffer_rsrc((void*)idx, 0, nb * (NF * 4), 0x00020000);
  const auto ors = __builtin_amdgcn_make_buffer_rsrc((void*)out, 0, nb * 2, 0x00020000);
  const unsigned f = 4 * c + g;
  const unsigned oob = f < NF ? 0u : 0x80000000u;
  const unsigned ix = __builtin_amdgcn_raw_buffer_load_b32(irs, ((b * NF + f) * 4) | oob, 0, 0);
  __builtin_amdgcn_sched_barrier(0);
  asm volatile("s_memrealtime %0" : "=s"(ts3) : "v"(ix) : "memory");
  __builtin_amdgcn_sched_barrier(0);
  const unsigned sh = (ix << 8) | oob, c16 = c * 16;
  u32x4 r[7];
#define LD(k) r[k] = __builtin_bit_cast(u32x4, __builtin_amdgcn_raw_buffer_load_b128(trs, bcast_or<k>(sh, c16), 0, 0));
  LD(0) LD(1) LD(2) LD(3) LD(4) LD(5) LD(6)
#undef LD
  __builtin_amdgcn_sched_barrier(0);
  const unsigned half = (c & 1) ? 0x3f000000u : 0x00003f00u;
  const unsigned jj = (c & 7) >> 1;
  const u32x4 a = {jj == 0 ? half : 0u, jj == 1 ? half : 0u, jj == 2 ? half : 0u, jj == 3 ? half : 0u};
  const bf16x8_t sel = __builtin_bit_cast(bf16x8_t, a);
  const unsigned di = c - 4 * g;
  const float w0 = di == 0 ? -0.5f : 0.f, w1 = di == 1 ? -0.5f : 0.f, w2 = di == 2 ? -0.5f : 0.f, w3 = di == 3 ? -0.5f : 0.f;
  const unsigned ooff = lane == 0 ? b * 2 : 0x80000000u;
  f32x4 acc = {0.f, 0.f, 0.f, 0.f}, dq = {0.f, 0.f, 0.f, 0.f};
  __builtin_amdgcn_sched_barrier(0);
#pragma unroll
  for (int k = 0; k < 7; ++k) {
    const bf16x8_t x = __builtin_bit_cast(bf16x8_t, r[k]);
    acc = __builtin_amdgcn_mfma_f32_16x16x32_bf16(sel, x, acc, 0, 0, 0);
    dq = __builtin_amdgcn_mfma_f32_16x16x32_bf16(x, x, dq, 0, 0, 0);
  }
  float t0 = acc[0] * acc[0], t1 = acc[1] * acc[1];
  t0 = fmaf(acc[2], acc[2], t0); t1 = fmaf(acc[3], acc[3], t1);
  t0 = fmaf(w0, dq[0], t0); t1 = fmaf(w1, dq[1], t1);
  t0 = fmaf(w2, dq[2], t0); t1 = fmaf(w3, dq[3], t1);
  float t = t0 + t1;
  DPP_ADD(t, 0x128); DPP_ADD(t, 0x124); DPP_ADD(t, 0x122); DPP_ADD(t, 0x121);
  const auto s16 = __builtin_amdgcn_permlane16_swap(__float_as_uint(t), __float_as_uint(t), false, false);
  t = __uint_as_float(s16[0]) + __uint_as_float(s16[1]);
  const auto s32 = __builtin_amdgcn_permlane32_swap(__float_as_uint(t), __float_as_uint(t), false, false);
  t = __uint_as_float(s32[0]) + __uint_as_float(s32[1]);
  const __hip_bfloat16 o = __float2bfloat16(t);
  __builtin_amdgcn_raw_buffer_store_b16(__builtin_bit_cast(unsigned short, o), ors, ooff, 0, 0);
  asm volatile("s_memrealtime %0\n\ts_waitcnt lgkmcnt(0)" : "=s"(ts4) : "v"(t) : "memory");
  if (lane < 8) {
    unsigned v = lane == 0 ? (unsigned)ts0 : lane == 1 ? (unsigned)ts1 : lane == 2 ? (unsigned)tsA : lane == 3 ? (unsigned)ts2 : lane == 4 ? (unsigned)tsB : lane == 5 ? (unsigned)ts3 : (unsigned)ts4;
    __hip_atomic_store(st + 8 * b + lane, v, __ATOMIC_RELAXED, __HIP_MEMORY_SCOPE_AGENT);
  }
  if (blockIdx.x == 0 && __builtin_amdgcn_readfirstlane(tid >> 6) == 0) {
    while (__builtin_amdgcn_s_memrealtime() < ts0 + 1500) __builtin_amdgcn_s_sleep(1);
    int sum = 0, smin = 1 << 30;
    for (int i = lane; i < 4096; i += 64) {
      unsigned x0 = __hip_atomic_load(st + 8 * i, __ATOMIC_RELAXED, __HIP_MEMORY_SCOPE_AGENT);
      unsigned lo = __hip_atomic_load(st + 8 * i + 0, __ATOMIC_RELAXED, __HIP_MEMORY_SCOPE_AGENT);
      unsigned hi = __hip_atomic_load(st + 8 * i + 1, __ATOMIC_RELAXED, __HIP_MEMORY_SCOPE_AGENT);
      int d = (int)(hi - lo); d = d < 0 ? 0 : d > 1500 ? 1500 : d;
      int a = (int)(x0 - (unsigned)ts0); a = a < -1000 ? -1000 : a;
      sum += d; smin = min(smin, a);
    }
    for (int o_ = 32; o_ > 0; o_ >>= 1) { sum += __shfl_xor(sum, o_); smin = min(smin, __shfl_xor(smin, o_)); }
    int val = __builtin_amdgcn_readfirstlane(sum * 20 / 4096);
    unsigned long long tend = ts0 + (long long)__builtin_amdgcn_readfirstlane(smin) + 5000 + (long long)val;
    while (__builtin_amdgcn_s_memrealtime() < tend) __builtin_amdgcn_s_sleep(1);
  }
}

extern "C" void kernel_launch(void* const* d_in, const int* in_sizes, int n_in, void* d_out, int out_size, void* d_ws,
                              size_t ws_size, hipStream_t stream) {
  const int* idx = (const int*)d_in[0];
  const unsigned char* table = (const unsigned char*)d_in[1];
  const unsigned nb = (unsigned)out_size;
  const unsigned spb = BLOCK / 64;
  const unsigned grid = (nb + spb - 1) / spb;
  fm_kernel<<<grid, BLOCK, 0, stream>>>(idx, table, (__hip_bfloat16*)d_out, (unsigned)in_sizes[1] * 2u, nb, (unsigned*)d_ws);
}
```

```cpp
#include <hip/hip_runtime.h>
#include <hip/hip_bf16.h>
#include <stdint.h>

typedef __attribute__((ext_vector_type(4))) unsigned int u32x4;
typedef __attribute__((ext_vector_type(4))) float f32x4;
typedef __attribute__((ext_vector_type(8))) __bf16 bf16x8_t;

#define BLOCK 512
#define NF 26

#define DPP_ADD(v, ctrl) v += __int_as_float(__builtin_amdgcn_update_dpp(0, __float_as_int(v), ctrl, 0xf, 0xf, true))

template <int K> __device__ __forceinline__ unsigned bcast_or(unsigned sh, unsigned c16) {
  return (unsigned)__builtin_amdgcn_update_dpp(0, (int)sh, 0x150 + K, 0xf, 0xf, true) | c16;
}

__global__ __launch_bounds__(BLOCK) void fm_kernel(const int* idx_, const unsigned char* table_,
                                                 __hip_bfloat16* out_, unsigned table_bytes_, unsigned nb_, unsigned* st_) {
  unsigned long long ts0, ts1, tsA, ts2, tsB, ts3, ts4;
  typedef const __attribute__((address_space(4))) unsigned long long* kp_t;
  kp_t kp = (kp_t)__builtin_amdgcn_kernarg_segment_ptr();
  asm volatile("s_memrealtime %0\n\t.p2align 6\n\ts_memrealtime %1\n\t.p2align 7\n\ts_memrealtime %2\n\ts_waitcnt lgkmcnt(0)\n\ts_memrealtime %3" : "=s"(ts0), "=s"(ts1), "=s"(tsA), "=s"(ts2), "+s"(kp) :: "memory");
  const int* idx = (const int*)kp[0]; const unsigned char* table = (const unsigned char*)kp[1]; __hip_bfloat16* out = (__hip_bfloat16*)kp[2];
  const unsigned table_bytes = (unsigned)kp[3], nb = (unsigned)(kp[3] >> 32); unsigned* st = (unsigned*)kp[4];
  __builtin_amdgcn_sched_barrier(0);
  asm volatile("s_memrealtime %0" : "=s"(tsB) : "s"(idx), "s"(table), "s"(nb), "s"(st) : "memory");
  __builtin_amdgcn_sched_barrier(0);
  const unsigned tid = threadIdx.x;
  const unsigned b = blockIdx.x * (BLOCK / 64) + (tid >> 6);
  const unsigned lane = tid & 63, c = tid & 15, g = lane >> 4;
  const auto trs = __builtin_amdgcn_make_buffer_rsrc((void*)table, 0, table_bytes, 0x00020000);
  const auto irs = __builtin_amdgcn_make_buffer_rsrc((void*)idx, 0, nb * (NF * 4), 0x00020000);
  const auto ors = __builtin_amdgcn_make_buffer_rsrc((void*)out, 0, nb * 2, 0x00020000);
  const unsigned f = 4 * c + g;
  const unsigned oob = f < NF ? 0u : 0x80000000u;
  const unsigned ix = __builtin_amdgcn_raw_buffer_load_b32(irs, ((b * NF + f) * 4) | oob, 0, 0);
  __builtin_amdgcn_sched_barrier(0);
  asm volatile("s_memrealtime %0" : "=s"(ts3) : "v"(ix) : "memory");
  __builtin_amdgcn_sched_barrier(0);
  const unsigned sh = (ix << 8) | oob, c16 = c * 16;
  u32x4 r[7];
#define LD(k) r[k] = __builtin_bit_cast(u32x4, __builtin_amdgcn_raw_buffer_load_b128(trs, bcast_or<k>(sh, c16), 0, 0));
  LD(0) LD(1) LD(2) LD(3) LD(4) LD(5) LD(6)
#undef LD
  __builtin_amdgcn_sched_barrier(0);
  const unsigned half = (c & 1) ? 0x3f000000u : 0x00003f00u;
  const unsigned jj = (c & 7) >> 1;
  const u32x4 a = {jj == 0 ? half : 0u, jj == 1 ? half : 0u, jj == 2 ? half : 0u, jj == 3 ? half : 0u};
  const bf16x8_t sel = __builtin_bit_cast(bf16x8_t, a);
  const unsigned di = c - 4 * g;
  const float w0 = di == 0 ? -0.5f : 0.f, w1 = di == 1 ? -0.5f : 0.f, w2 = di == 2 ? -0.5f : 0.f, w3 = di == 3 ? -0.5f : 0.f;
  const unsigned ooff = lane == 0 ? b * 2 : 0x80000000u;
  f32x4 acc = {0.f, 0.f, 0.f, 0.f}, dq = {0.f, 0.f, 0.f, 0.f};
  __builtin_amdgcn_sched_barrier(0);
#pragma unroll
  for (int k = 0; k < 7; ++k) {
    const bf16x8_t x = __builtin_bit_cast(bf16x8_t, r[k]);
    acc = __builtin_amdgcn_mfma_f32_16x16x32_bf16(sel, x, acc, 0, 0, 0);
    dq = __builtin_amdgcn_mfma_f32_16x16x32_bf16(x, x, dq, 0, 0, 0);
  }
  float t0 = acc[0] * acc[0], t1 = acc[1] * acc[1];
  t0 = fmaf(acc[2], acc[2], t0); t1 = fmaf(acc[3], acc[3], t1);
  t0 = fmaf(w0, dq[0], t0); t1 = fmaf(w1, dq[1], t1);
  t0 = fmaf(w2, dq[2], t0); t1 = fmaf(w3, dq[3], t1);
  float t = t0 + t1;
  DPP_ADD(t, 0x128); DPP_ADD(t, 0x124); DPP_ADD(t, 0x122); DPP_ADD(t, 0x121);
  const auto s16 = __builtin_amdgcn_permlane16_swap(__float_as_uint(t), __float_as_uint(t), false, false);
  t = __uint_as_float(s16[0]) + __uint_as_float(s16[1]);
  const auto s32 = __builtin_amdgcn_permlane32_swap(__float_as_uint(t), __float_as_uint(t), false, false);
  t = __uint_as_float(s32[0]) + __uint_as_float(s32[1]);
  const __hip_bfloat16 o = __float2bfloat16(t);
  __builtin_amdgcn_raw_buffer_store_b16(__builtin_bit_cast(unsigned short, o), ors, ooff, 0, 0);
  asm volatile("s_memrealtime %0\n\ts_waitcnt lgkmcnt(0)" : "=s"(ts4) : "v"(t) : "memory");
  if (lane < 8) {
    unsigned v = lane == 0 ? (unsigned)ts0 : lane == 1 ? (unsigned)ts1 : lane == 2 ? (unsigned)tsA : lane == 3 ? (unsigned)ts2 : lane == 4 ? (unsigned)tsB : lane == 5 ? (unsigned)ts3 : (unsigned)ts4;
    __hip_atomic_store(st + 8 * b + lane, v, __ATOMIC_RELAXED, __HIP_MEMORY_SCOPE_AGENT);
  }
  if (blockIdx.x == 0 && __builtin_amdgcn_readfirstlane(tid >> 6) == 0) {
    while (__builtin_amdgcn_s_memrealtime() < ts0 + 1500) __builtin_amdgcn_s_sleep(1);
    int sum = 0, smin = 1 << 30;
    for (int i = lane; i < 4096; i += 64) {
      unsigned x0 = __hip_atomic_load(st + 8 * i, __ATOMIC_RELAXED, __HIP_MEMORY_SCOPE_AGENT);
      unsigned lo = __hip_atomic_load(st + 8 * i + 3, __ATOMIC_RELAXED, __HIP_MEMORY_SCOPE_AGENT);
      unsigned hi = __hip_atomic_load(st + 8 * i + 4, __ATOMIC_RELAXED, __HIP_MEMORY_SCOPE_AGENT);
      int d = (int)(hi - lo); d = d < 0 ? 0 : d > 1500 ? 1500 : d;
      int a = (int)(x0 - (unsigned)ts0); a = a < -1000 ? -1000 : a;
      sum = max(sum, d); smin = min(smin, a);
    }
    for (int o_ = 32; o_ > 0; o_ >>= 1) { sum = max(sum, __shfl_xor(sum, o_)); smin = min(smin, __shfl_xor(smin, o_)); }
    int val = __builtin_amdgcn_readfirstlane(sum * 20);
    unsigned long long tend = ts0 + (long long)__builtin_amdgcn_readfirstlane(smin) + 5000 + (long long)val;
    while (__builtin_amdgcn_s_memrealtime() < tend) __builtin_amdgcn_s_sleep(1);
  }
}

extern "C" void kernel_launch(void* const* d_in, const int* in_sizes, int n_in, void* d_out, int out_size, void* d_ws,
                              size_t ws_size, hipStream_t stream) {
  const int* idx = (const int*)d_in[0];
  const unsigned char* table = (const unsigned char*)d_in[1];
  const unsigned nb = (unsigned)out_size;
  const unsigned spb = BLOCK / 64;
  const unsigned grid = (nb + spb - 1) / spb;
  fm_kernel<<<grid, BLOCK, 0, stream>>>(idx, table, (__hip_bfloat16*)d_out, (unsigned)in_sizes[1] * 2u, nb, (unsigned*)d_ws);
}
```

```cpp
#include <hip/hip_runtime.h>
#include <hip/hip_bf16.h>
#include <stdint.h>

typedef __attribute__((ext_vector_type(4))) unsigned int u32x4;
typedef __attribute__((ext_vector_type(4))) float f32x4;
typedef __attribute__((ext_vector_type(8))) __bf16 bf16x8_t;

#define BLOCK 512
#define NF 26

#define DPP_ADD(v, ctrl) v += __int_as_float(__builtin_amdgcn_update_dpp(0, __float_as_int(v), ctrl, 0xf, 0xf, true))

template <int K> __device__ __forceinline__ unsigned bcast_or(unsigned sh, unsigned c16) {
  return (unsigned)__builtin_amdgcn_update_dpp(0, (int)sh, 0x150 + K, 0xf, 0xf, true) | c16;
}

__global__ __launch_bounds__(BLOCK) void fm_kernel(const int* __restrict__ idx, const unsigned char* __restrict__ table,
                                                 __hip_bfloat16* __restrict__ out, unsigned table_bytes, unsigned nb) {
  const unsigned tid = threadIdx.x;
  const unsigned b = blockIdx.x * (BLOCK / 64) + (tid >> 6);
  const unsigned lane = tid & 63, c = tid & 15, g = lane >> 4;
  const auto trs = __builtin_amdgcn_make_buffer_rsrc((void*)table, 0, table_bytes, 0x00020000);
  const auto irs = __builtin_amdgcn_make_buffer_rsrc((void*)idx, 0, nb * (NF * 4), 0x00020000);
  const auto ors = __builtin_amdgcn_make_buffer_rsrc((void*)out, 0, nb * 2, 0x00020000);
  const unsigned f = 4 * c + g;
  const unsigned oob = f < NF ? 0u : 0x80000000u;
  const unsigned ix = __builtin_amdgcn_raw_buffer_load_b32(irs, ((b * NF + f) * 4) | oob, 0, 0);
  const unsigned sh = (ix << 8) | oob, c16 = c * 16;
  u32x4 r[7];
#define LD(k) r[k] = __builtin_bit_cast(u32x4, __builtin_amdgcn_raw_buffer_load_b128(trs, bcast_or<k>(sh, c16), 0, 0));
  LD(0) LD(1) LD(2) LD(3) LD(4) LD(5) LD(6)
#undef LD
  __builtin_amdgcn_sched_barrier(0);
  const unsigned half = (c & 1) ? 0x3f000000u : 0x00003f00u;
  const unsigned jj = (c & 7) >> 1;
  const u32x4 a = {jj == 0 ? half : 0u, jj == 1 ? half : 0u, jj == 2 ? half : 0u, jj == 3 ? half : 0u};
  const bf16x8_t sel = __builtin_bit_cast(bf16x8_t, a);
  const unsigned di = c - 4 * g;
  const float w0 = di == 0 ? -0.5f : 0.f, w1 = di == 1 ? -0.5f : 0.f, w2 = di == 2 ? -0.5f : 0.f, w3 = di == 3 ? -0.5f : 0.f;
  const unsigned ooff = lane == 0 ? b * 2 : 0x80000000u;
  f32x4 acc = {0.f, 0.f, 0.f, 0.f}, dq = {0.f, 0.f, 0.f, 0.f};
  __builtin_amdgcn_sched_barrier(0);
#pragma unroll
  for (int k = 0; k < 7; ++k) {
    const bf16x8_t x = __builtin_bit_cast(bf16x8_t, r[k]);
    acc = __builtin_amdgcn_mfma_f32_16x16x32_bf16(sel, x, acc, 0, 0, 0);
    dq = __builtin_amdgcn_mfma_f32_16x16x32_bf16(x, x, dq, 0, 0, 0);
  }
  float t0 = acc[0] * acc[0], t1 = acc[1] * acc[1];
  t0 = fmaf(acc[2], acc[2], t0); t1 = fmaf(acc[3], acc[3], t1);
  t0 = fmaf(w0, dq[0], t0); t1 = fmaf(w1, dq[1], t1);
  t0 = fmaf(w2, dq[2], t0); t1 = fmaf(w3, dq[3], t1);
  float t = t0 + t1;
  DPP_ADD(t, 0x128); DPP_ADD(t, 0x124); DPP_ADD(t, 0x122); DPP_ADD(t, 0x121);
  const auto s16 = __builtin_amdgcn_permlane16_swap(__float_as_uint(t), __float_as_uint(t), false, false);
  t = __uint_as_float(s16[0]) + __uint_as_float(s16[1]);
  const auto s32 = __builtin_amdgcn_permlane32_swap(__float_as_uint(t), __float_as_uint(t), false, false);
  t = __uint_as_float(s32[0]) + __uint_as_float(s32[1]);
  const __hip_bfloat16 o = __float2bfloat16(t);
  __builtin_amdgcn_raw_buffer_store_b16(__builtin_bit_cast(unsigned short, o), ors, ooff, 0, 0);
}

extern "C" void kernel_launch(void* const* d_in, const int* in_sizes, int n_in, void* d_out, int out_size, void* d_ws,
                              size_t ws_size, hipStream_t stream) {
  const int* idx = (const int*)d_in[0];
  const unsigned char* table = (const unsigned char*)d_in[1];
  const unsigned nb = (unsigned)out_size;
  const unsigned spb = BLOCK / 64;
  const unsigned grid = (nb + spb - 1) / spb;
  fm_kernel<<<grid, BLOCK, 0, stream>>>(idx, table, (__hip_bfloat16*)d_out, (unsigned)in_sizes[1] * 2u, nb);
}
```
